```python
import torch

def _softmax(x, axis=-1):
    return torch.softmax(x, dim=axis)
def _gelu(x):
    return torch.nn.functional.gelu(x, approximate="tanh")
def _silu(x):
    return torch.nn.functional.silu(x)
def _sigmoid(x):
    return torch.sigmoid(x)
def _layernorm(x, axis=-1, eps=1e-5):
    m = x.mean(dim=axis, keepdim=True)
    v = x.var(dim=axis, keepdim=True, unbiased=False)
    return (x - m) / torch.sqrt(v + eps)
def _scatter_add(tgt, idx, src, axis=0):
    return tgt.clone().index_add_(axis, idx, src)
def _segment_sum(src, seg, n):
    out = torch.zeros((n,) + src.shape[1:], dtype=src.dtype)
    return out.index_add_(0, seg, src)
def _pad(x, pw):
    flat = tuple(v for pair in reversed(pw) for v in pair)
    return torch.nn.functional.pad(x, flat)

kernel_name = 'standalone__grn__sampling'

def setup_inputs():

    _w = (lambda a: (lambda t: t.to(torch.bfloat16) if t.is_floating_point() else t)(torch.as_tensor(a)))
    B, S, D = 1024, 16, 640
    return {
        "x": _w((lambda *s: torch.randn(*s, dtype=torch.bfloat16) * 0.05)(B, S, D)),
        "g": _w((lambda *s: torch.randn(*s, dtype=torch.bfloat16) * 0.4)(D) * 0.1),
        "b": _w((lambda *s: torch.randn(*s, dtype=torch.bfloat16) * 0.1)(D) * 0.1),
    }

def reference(x, g, b):
    gx = torch.sqrt((x * x).sum(axis=-2, keepdims=True))
    nx = gx / (gx.mean(axis=-1, keepdims=True) + 1e-6)
    return g * (x * nx) + b + x
```

```cpp
#include <hip/hip_runtime.h>
#include <hip/hip_bf16.h>

typedef unsigned int u32;
typedef u32 u32x4 __attribute__((ext_vector_type(4)));
typedef float f32x2 __attribute__((ext_vector_type(2)));
typedef float f32x4 __attribute__((ext_vector_type(4)));
typedef __bf16 bf16x2 __attribute__((ext_vector_type(2)));

#define LOAD_AUX 19
#define STORE_AUX 19

static constexpr int S_ = 16;
static constexpr int D_ = 640;
static constexpr int ROWB = D_ * 2;
static constexpr int BATB = S_ * ROWB;

__device__ __forceinline__ float lo_f(u32 v) { return __builtin_bit_cast(float, v << 16); }
__device__ __forceinline__ float hi_f(u32 v) { return __builtin_bit_cast(float, v & 0xffff0000u); }
__device__ __forceinline__ u32 fbits(float f) { return __builtin_bit_cast(u32, f); }
__device__ __forceinline__ float bitsf(u32 u) { return __builtin_bit_cast(float, u); }
__device__ __forceinline__ u32 pack_bf16(float lo, float hi) {
  f32x2 v = {lo, hi};
  bf16x2 r = __builtin_convertvector(v, bf16x2);
  return __builtin_bit_cast(u32, r);
}
template <int CTRL>
__device__ __forceinline__ float dpp_add(float v) {
  return v + bitsf(__builtin_amdgcn_update_dpp(0u, fbits(v), CTRL, 0xf, 0xf, true));
}

__global__ __launch_bounds__(320) void grn_kernel(const void* __restrict__ x, const void* __restrict__ g,
                                                  const void* __restrict__ b, void* __restrict__ y) {
  __shared__ __attribute__((aligned(16))) float red[8];
  __builtin_amdgcn_s_setprio(3);
  const u32 t = threadIdx.x;
  const u32 lane = t & 63u;
  const u32 w = t >> 6;
  const u32 q = lane >> 4;
  const u32 colb = (w * 16u + (lane & 15u)) * 16u;
  const u32 voff = q * (4u * ROWB) + colb;
  const size_t boff = (size_t)blockIdx.x * (size_t)BATB;

  auto rx = __builtin_amdgcn_make_buffer_rsrc((char*)x + boff, 0, BATB, 0x00020000);
  auto ry = __builtin_amdgcn_make_buffer_rsrc((char*)y + boff, 0, BATB, 0x00020000);
  auto rg = __builtin_amdgcn_make_buffer_rsrc((void*)g, 0, ROWB, 0x00020000);
  auto rb = __builtin_amdgcn_make_buffer_rsrc((void*)b, 0, ROWB, 0x00020000);

  u32x4 xv[4];
#pragma unroll
  for (int i = 0; i < 4; ++i) xv[i] = __builtin_amdgcn_raw_buffer_load_b128(rx, voff + i * ROWB, 0, LOAD_AUX);
  const u32 gv = __builtin_amdgcn_raw_buffer_load_b32(rg, colb + q * 4u, 0, 0);
  const u32x4 bv = __builtin_amdgcn_raw_buffer_load_b128(rb, colb, 0, 0);
  __builtin_amdgcn_sched_barrier(0);
  __builtin_amdgcn_s_setprio(0);
  __builtin_amdgcn_sched_barrier(0);

  float xf[4][8];
  float p[8];
#pragma unroll
  for (int i = 0; i < 4; ++i) {
#pragma unroll
    for (int k = 0; k < 4; ++k) {
      xf[i][2 * k] = lo_f(xv[i][k]);
      xf[i][2 * k + 1] = hi_f(xv[i][k]);
    }
#pragma unroll
    for (int e = 0; e < 8; ++e) p[e] = (i == 0) ? xf[i][e] * xf[i][e] : __builtin_fmaf(xf[i][e], xf[i][e], p[e]);
  }
  float s1[4];
#pragma unroll
  for (int e = 0; e < 4; ++e) {
    auto r = __builtin_amdgcn_permlane32_swap(fbits(p[e]), fbits(p[e + 4]), false, false);
    s1[e] = bitsf(r[0]) + bitsf(r[1]);
  }
  float gx[2];
#pragma unroll
  for (int e = 0; e < 2; ++e) {
    auto r = __builtin_amdgcn_permlane16_swap(fbits(s1[e]), fbits(s1[e + 2]), false, false);
    gx[e] = __builtin_amdgcn_sqrtf(bitsf(r[0]) + bitsf(r[1]));
  }
  float tot = gx[0] + gx[1];
  tot = dpp_add<0xB1>(tot);
  tot = dpp_add<0x4E>(tot);
  tot = dpp_add<0x141>(tot);
  tot = dpp_add<0x140>(tot);
  {
    auto r = __builtin_amdgcn_permlane16_swap(fbits(tot), fbits(tot), false, false);
    tot = bitsf(r[0]) + bitsf(r[1]);
    auto r2 = __builtin_amdgcn_permlane32_swap(fbits(tot), fbits(tot), false, false);
    tot = bitsf(r2[0]) + bitsf(r2[1]);
  }
  if (lane == 0) red[w] = tot;
  const float u2[2] = {lo_f(gv) * gx[0], hi_f(gv) * gx[1]};
  float t4[4];
#pragma unroll
  for (int e = 0; e < 2; ++e) {
    auto r = __builtin_amdgcn_permlane16_swap(fbits(u2[e]), fbits(u2[e]), false, false);
    t4[e] = bitsf(r[0]);
    t4[e + 2] = bitsf(r[1]);
  }
  float u[8];
#pragma unroll
  for (int e = 0; e < 4; ++e) {
    auto r = __builtin_amdgcn_permlane32_swap(fbits(t4[e]), fbits(t4[e]), false, false);
    u[e] = bitsf(r[0]);
    u[e + 4] = bitsf(r[1]);
  }
  float bf[8];
#pragma unroll
  for (int k = 0; k < 4; ++k) {
    bf[2 * k] = lo_f(bv[k]);
    bf[2 * k + 1] = hi_f(bv[k]);
  }
  __syncthreads();
  __builtin_amdgcn_sched_barrier(0);
  const f32x4 r4 = *(const f32x4*)red;
  const float total = ((r4[0] + r4[1]) + (r4[2] + r4[3])) + red[4];
  const float inv = __builtin_amdgcn_rcpf(total * (1.0f / D_) + 1e-6f);
  float sc[8];
#pragma unroll
  for (int e = 0; e < 8; ++e) sc[e] = __builtin_fmaf(u[e], inv, 1.0f);
#pragma unroll
  for (int i = 0; i < 4; ++i) {
    u32x4 o;
#pragma unroll
    for (int k = 0; k < 4; ++k)
      o[k] = pack_bf16(__builtin_fmaf(xf[i][2 * k], sc[2 * k], bf[2 * k]),
                       __builtin_fmaf(xf[i][2 * k + 1], sc[2 * k + 1], bf[2 * k + 1]));
    __builtin_amdgcn_raw_buffer_store_b128(o, ry, voff + i * ROWB, 0, STORE_AUX);
  }
}

extern "C" void kernel_launch(void* const* d_in, const int* in_sizes, int n_in, void* d_out, int out_size,
                              void* d_ws, size_t ws_size, hipStream_t stream) {
  const int nb = in_sizes[0] / (S_ * D_);
  grn_kernel<<<nb, 320, 0, stream>>>(d_in[0], d_in[1], d_in[2], d_out);
}
```

```cpp
#include <hip/hip_runtime.h>
#include <hip/hip_bf16.h>

typedef unsigned int u32;
typedef u32 u32x4 __attribute__((ext_vector_type(4)));
typedef float f32x2 __attribute__((ext_vector_type(2)));
typedef float f32x4 __attribute__((ext_vector_type(4)));
typedef __bf16 bf16x2 __attribute__((ext_vector_type(2)));

#define LOAD_AUX 19
#define STORE_AUX 19

static constexpr u32 GRP = 32;
static constexpr int S_ = 16;
static constexpr int D_ = 640;
static constexpr int ROWB = D_ * 2;
static constexpr int BATB = S_ * ROWB;

__device__ __forceinline__ float lo_f(u32 v) { return __builtin_bit_cast(float, v << 16); }
__device__ __forceinline__ float hi_f(u32 v) { return __builtin_bit_cast(float, v & 0xffff0000u); }
__device__ __forceinline__ u32 fbits(float f) { return __builtin_bit_cast(u32, f); }
__device__ __forceinline__ float bitsf(u32 u) { return __builtin_bit_cast(float, u); }
__device__ __forceinline__ u32 pack_bf16(float lo, float hi) {
  f32x2 v = {lo, hi};
  bf16x2 r = __builtin_convertvector(v, bf16x2);
  return __builtin_bit_cast(u32, r);
}
template <int CTRL>
__device__ __forceinline__ float dpp_add(float v) {
  return v + bitsf(__builtin_amdgcn_update_dpp(0u, fbits(v), CTRL, 0xf, 0xf, true));
}

__global__ __launch_bounds__(320) void grn_kernel(const void* __restrict__ x, const void* __restrict__ g,
                                                  const void* __restrict__ b, void* __restrict__ y, u32 nwin) {
  __shared__ __attribute__((aligned(16))) float red[8];
  __builtin_amdgcn_s_setprio(3);
  const u32 t = threadIdx.x;
  const u32 lane = t & 63u;
  const u32 w = t >> 6;
  const u32 q = lane >> 4;
  const u32 colb = (w * 16u + (lane & 15u)) * 16u;
  const u32 voff = q * (4u * ROWB) + colb;
  const u32 bi = blockIdx.x;
  const u32 bm = (bi & ~(8u * GRP - 1u)) + (bi & 7u) * GRP + ((bi & (8u * GRP - 1u)) >> 3);
  const u32 be = bi < nwin ? bm : bi;
  const size_t boff = (size_t)be * (size_t)BATB;

  auto rx = __builtin_amdgcn_make_buffer_rsrc((char*)x + boff, 0, BATB, 0x00020000);
  auto ry = __builtin_amdgcn_make_buffer_rsrc((char*)y + boff, 0, BATB, 0x00020000);
  auto rg = __builtin_amdgcn_make_buffer_rsrc((void*)g, 0, ROWB, 0x00020000);
  auto rb = __builtin_amdgcn_make_buffer_rsrc((void*)b, 0, ROWB, 0x00020000);

  u32x4 xv[4];
#pragma unroll
  for (int i = 0; i < 4; ++i) xv[i] = __builtin_amdgcn_raw_buffer_load_b128(rx, voff + i * ROWB, 0, LOAD_AUX);
  const u32 gv = __builtin_amdgcn_raw_buffer_load_b32(rg, colb + q * 4u, 0, 0);
  const u32x4 bv = __builtin_amdgcn_raw_buffer_load_b128(rb, colb, 0, 0);
  __builtin_amdgcn_sched_barrier(0);
  __builtin_amdgcn_s_setprio(0);
  __builtin_amdgcn_sched_barrier(0);

  float xf[4][8];
  float p[8];
#pragma unroll
  for (int i = 0; i < 4; ++i) {
#pragma unroll
    for (int k = 0; k < 4; ++k) {
      xf[i][2 * k] = lo_f(xv[i][k]);
      xf[i][2 * k + 1] = hi_f(xv[i][k]);
    }
#pragma unroll
    for (int e = 0; e < 8; ++e) p[e] = (i == 0) ? xf[i][e] * xf[i][e] : __builtin_fmaf(xf[i][e], xf[i][e], p[e]);
  }
  float s1[4];
#pragma unroll
  for (int e = 0; e < 4; ++e) {
    auto r = __builtin_amdgcn_permlane32_swap(fbits(p[e]), fbits(p[e + 4]), false, false);
    s1[e] = bitsf(r[0]) + bitsf(r[1]);
  }
  float gx[2];
#pragma unroll
  for (int e = 0; e < 2; ++e) {
    auto r = __builtin_amdgcn_permlane16_swap(fbits(s1[e]), fbits(s1[e + 2]), false, false);
    gx[e] = __builtin_amdgcn_sqrtf(bitsf(r[0]) + bitsf(r[1]));
  }
  float tot = gx[0] + gx[1];
  tot = dpp_add<0xB1>(tot);
  tot = dpp_add<0x4E>(tot);
  tot = dpp_add<0x141>(tot);
  tot = dpp_add<0x140>(tot);
  {
    auto r = __builtin_amdgcn_permlane16_swap(fbits(tot), fbits(tot), false, false);
    tot = bitsf(r[0]) + bitsf(r[1]);
    auto r2 = __builtin_amdgcn_permlane32_swap(fbits(tot), fbits(tot), false, false);
    tot = bitsf(r2[0]) + bitsf(r2[1]);
  }
  if (lane == 0) red[w] = tot;
  const float u2[2] = {lo_f(gv) * gx[0], hi_f(gv) * gx[1]};
  float t4[4];
#pragma unroll
  for (int e = 0; e < 2; ++e) {
    auto r = __builtin_amdgcn_permlane16_swap(fbits(u2[e]), fbits(u2[e]), false, false);
    t4[e] = bitsf(r[0]);
    t4[e + 2] = bitsf(r[1]);
  }
  float u[8];
#pragma unroll
  for (int e = 0; e < 4; ++e) {
    auto r = __builtin_amdgcn_permlane32_swap(fbits(t4[e]), fbits(t4[e]), false, false);
    u[e] = bitsf(r[0]);
    u[e + 4] = bitsf(r[1]);
  }
  float bf[8];
#pragma unroll
  for (int k = 0; k < 4; ++k) {
    bf[2 * k] = lo_f(bv[k]);
    bf[2 * k + 1] = hi_f(bv[k]);
  }
  __syncthreads();
  __builtin_amdgcn_sched_barrier(0);
  const f32x4 r4 = *(const f32x4*)red;
  const float total = ((r4[0] + r4[1]) + (r4[2] + r4[3])) + red[4];
  const float inv = __builtin_amdgcn_rcpf(total * (1.0f / D_) + 1e-6f);
  float sc[8];
#pragma unroll
  for (int e = 0; e < 8; ++e) sc[e] = __builtin_fmaf(u[e], inv, 1.0f);
#pragma unroll
  for (int i = 0; i < 4; ++i) {
    u32x4 o;
#pragma unroll
    for (int k = 0; k < 4; ++k)
      o[k] = pack_bf16(__builtin_fmaf(xf[i][2 * k], sc[2 * k], bf[2 * k]),
                       __builtin_fmaf(xf[i][2 * k + 1], sc[2 * k + 1], bf[2 * k + 1]));
    __builtin_amdgcn_raw_buffer_store_b128(o, ry, voff + i * ROWB, 0, STORE_AUX);
  }
}

extern "C" void kernel_launch(void* const* d_in, const int* in_sizes, int n_in, void* d_out, int out_size,
                              void* d_ws, size_t ws_size, hipStream_t stream) {
  const int nb = in_sizes[0] / (S_ * D_);
  const u32 nwin = (u32)nb & ~(8u * GRP - 1u);
  grn_kernel<<<nb, 320, 0, stream>>>(d_in[0], d_in[1], d_in[2], d_out, nwin);
}
```
